```python
import torch

def _softmax(x, axis=-1):
    return torch.softmax(x, dim=axis)
def _gelu(x):
    return torch.nn.functional.gelu(x, approximate="tanh")
def _silu(x):
    return torch.nn.functional.silu(x)
def _sigmoid(x):
    return torch.sigmoid(x)
def _layernorm(x, axis=-1, eps=1e-5):
    m = x.mean(dim=axis, keepdim=True)
    v = x.var(dim=axis, keepdim=True, unbiased=False)
    return (x - m) / torch.sqrt(v + eps)
def _scatter_add(tgt, idx, src, axis=0):
    return tgt.clone().index_add_(axis, idx, src)
def _segment_sum(src, seg, n):
    out = torch.zeros((n,) + src.shape[1:], dtype=src.dtype)
    return out.index_add_(0, seg, src)
def _pad(x, pw):
    flat = tuple(v for pair in reversed(pw) for v in pair)
    return torch.nn.functional.pad(x, flat)

kernel_name = 'standalone__w8a8_smoothquant_per_tensor_per_channel_int8_activations_weights__sampling'

def setup_inputs():

    _w = (lambda a: (lambda t: t.to(torch.bfloat16) if t.is_floating_point() else t)(torch.as_tensor(a)))
    B, S, D, F = 32, 16, 1024, 3584
    x = abs((lambda *s: torch.randn(*s, dtype=torch.bfloat16) * 0.05)(B, S, D))
    w = (lambda *s: torch.randn(*s, dtype=torch.bfloat16) * 0.05)(D, F)
    x_min, x_max = float(x.min()), float(x.max())
    sx = (x_max - x_min) / 255.0 + 1e-6
    zx = int(torch.as_tensor(-128.0 - x_min / sx).round())
    qx = torch.clamp(torch.as_tensor(x / sx).round() + zx, -128, 127).to(torch.int32)
    sw = (torch.abs(w).amax(dim=0) / 127.0 + 1e-6).to(float)
    qw = torch.clamp(torch.as_tensor(w / sw[None, :]).round(), -127, 127).to(torch.int32)
    return {
        "qx": _w(qx),
        "qw": _w(qw),
        "scale_x": _w(torch.tensor(sx, dtype=float)),
        "zero_x": _w(torch.tensor(zx, dtype=torch.int32)),
        "scale_w": _w(sw),
        "bias": _w((lambda *s: torch.randn(*s, dtype=torch.bfloat16) * 0.05)(F)),
    }

def reference(qx, qw, scale_x, zero_x, scale_w, bias):
    dt = scale_w.dtype
    acc = torch.einsum("bsd,df->bsf", (lambda x, dt: x.to(dt))(qx, dt), (lambda x, dt: x.to(dt))(qw, dt))
    w_colsum = torch.sum((lambda x, dt: x.to(dt))(qw, dt), axis=0)
    acc = acc - (lambda x, dt: x.to(dt))(zero_x, dt) * w_colsum[None, None, :]
    return acc * scale_x * scale_w[None, None, :] + bias[None, None, :]
```

```cpp
#include <hip/hip_runtime.h>
#include <hip/hip_bf16.h>
#include <stdint.h>

typedef int v4i __attribute__((ext_vector_type(4)));
typedef unsigned v4u __attribute__((ext_vector_type(4)));
typedef unsigned v2u __attribute__((ext_vector_type(2)));

constexpr int M = 512, K = 1024, N = 3584;

__device__ __forceinline__ unsigned pack4(unsigned a, unsigned b, unsigned c, unsigned d) {
  unsigned t0 = __builtin_amdgcn_perm(b, a, 0x0c0c0400u);
  unsigned t1 = __builtin_amdgcn_perm(d, c, 0x04000c0cu);
  return t0 | t1;
}
__device__ __forceinline__ float bf16r(float x) {
  __bf16 h = (__bf16)x;
  return (float)h;
}
__device__ __forceinline__ float bf2f(unsigned short h) { return __uint_as_float((unsigned)h << 16); }

__global__ __launch_bounds__(512, 2) void w8a8_kernel(const int* __restrict__ qx, const int* __restrict__ qw,
                                                      const unsigned short* __restrict__ scale_x,
                                                      const int* __restrict__ zero_x,
                                                      const unsigned short* __restrict__ scale_w,
                                                      const unsigned short* __restrict__ bias,
                                                      unsigned short* __restrict__ out) {
  extern __shared__ __attribute__((aligned(16))) unsigned char smem[];
  v4u* Bs = (v4u*)smem;
  const int tid = threadIdx.x;
  const int lane = tid & 63;
  const int w = __builtin_amdgcn_readfirstlane(tid >> 6);
  v4u* Xs = (v4u*)(smem + 65536 + w * 4096);
  int* cs_lds = (int*)(smem + 65536 + 32768);

  const int b = blockIdx.x;
  const int xcd = b & 7, idx = b >> 3;
  const int ntile = xcd * 7 + (idx >> 2), mq = idx & 3;
  const int n0 = ntile * 64, m0 = mq * 128;

  const __amdgpu_buffer_rsrc_t rw = __builtin_amdgcn_make_buffer_rsrc((void*)qw, 0, K * N * 4, 0x00020000);
  const __amdgpu_buffer_rsrc_t rx = __builtin_amdgcn_make_buffer_rsrc((void*)qx, 0, M * K * 4, 0x00020000);

  if (tid < 64) cs_lds[tid] = 0;

  const int cq = lane & 15, q = lane >> 4;
  const unsigned wvoff = (unsigned)((128 * w + 4 * q) * N + n0 + 4 * cq) * 4u;
  v4u wv[2][16];
#pragma unroll
  for (int g = 0; g < 2; ++g)
#pragma unroll
    for (int j = 0; j < 16; ++j)
      wv[g][j] = __builtin_amdgcn_raw_buffer_load_b128(rw, wvoff, (32 * (j >> 2) + 16 * g + (j & 3)) * N * 4, 2);
  __builtin_amdgcn_sched_barrier(0);

  const int mg = w & 3, kh = w >> 2;
  const int rr = lane >> 3, s = lane & 7;
  const unsigned xvoff = (unsigned)((m0 + 32 * mg + rr) * K + 512 * kh + 4 * s) * 4u;
  v4u xv[2][16];
  int cs[4] = {0, 0, 0, 0};
  __syncthreads();
#pragma unroll
  for (int g = 0; g < 2; ++g) {
#pragma unroll
    for (int c = 0; c < 4; ++c) {
      v4u p;
#pragma unroll
      for (int d = 0; d < 4; ++d) {
        p[d] = pack4(wv[g][4 * d][c], wv[g][4 * d + 1][c], wv[g][4 * d + 2][c], wv[g][4 * d + 3][c]);
        cs[c] = __builtin_amdgcn_sdot4((int)p[d], 0x01010101, cs[c], false);
      }
      Bs[((2 * w + g) * 4 + c) * 64 + lane] = p;
    }
    __builtin_amdgcn_sched_barrier(0);
#pragma unroll
    for (int rg = 0; rg < 4; ++rg)
#pragma unroll
      for (int i = 0; i < 4; ++i)
        xv[g][rg * 4 + i] = __builtin_amdgcn_raw_buffer_load_b128(rx, xvoff + i * 128, (rg * 8 * K + g * 128) * 4, 0);
    __builtin_amdgcn_sched_barrier(0);
  }
#pragma unroll
  for (int c = 0; c < 4; ++c) atomicAdd(&cs_lds[4 * cq + c], cs[c]);
  __builtin_amdgcn_s_waitcnt(0xC07F);
  __builtin_amdgcn_s_barrier();
  __builtin_amdgcn_sched_barrier(0);

  v4i acc[2][4];
#pragma unroll
  for (int mt = 0; mt < 2; ++mt)
#pragma unroll
    for (int c = 0; c < 4; ++c) acc[mt][c] = (v4i){0, 0, 0, 0};

  const int r16 = lane & 15;
#pragma unroll
  for (int ch = 0; ch < 4; ++ch) {
    const int bu = ch & 1;
#pragma unroll
    for (int rg = 0; rg < 4; ++rg) {
      v4u p;
#pragma unroll
      for (int i = 0; i < 4; ++i) {
        v4u v = xv[bu][rg * 4 + i];
        p[i] = pack4(v[0], v[1], v[2], v[3]);
      }
      const int row = 8 * (rg & 1) + rr;
      Xs[((rg >> 1) * 2 + (s >> 2)) * 64 + 16 * (s & 3) + (row ^ s)] = p;
    }
    __builtin_amdgcn_sched_barrier(0);
    if (ch + 2 < 4) {
#pragma unroll
      for (int rg = 0; rg < 4; ++rg)
#pragma unroll
        for (int i = 0; i < 4; ++i)
          xv[bu][rg * 4 + i] =
              __builtin_amdgcn_raw_buffer_load_b128(rx, xvoff + i * 128, (rg * 8 * K + (ch + 2) * 128) * 4, 0);
    }
    __builtin_amdgcn_sched_barrier(0);
#pragma unroll
    for (int t = 0; t < 2; ++t) {
      const int kstep = (kh * 4 + ch) * 2 + t;
      v4u a[2], bb[4];
#pragma unroll
      for (int mt = 0; mt < 2; ++mt) a[mt] = Xs[(mt * 2 + t) * 64 + 16 * q + (r16 ^ (4 * t + q))];
#pragma unroll
      for (int c = 0; c < 4; ++c) bb[c] = Bs[(kstep * 4 + c) * 64 + lane];
#pragma unroll
      for (int mt = 0; mt < 2; ++mt)
#pragma unroll
        for (int c = 0; c < 4; ++c)
          acc[mt][c] = __builtin_amdgcn_mfma_i32_16x16x64_i8((v4i)a[mt], (v4i)bb[c], acc[mt][c], 0, 0, 0);
    }
    __builtin_amdgcn_sched_barrier(0);
  }

#pragma unroll
  for (int c = 0; c < 4; ++c) Xs[c * 64 + lane] = (v4u)(kh ? acc[0][c] : acc[1][c]);
  __syncthreads();
  const v4u* Xp = (const v4u*)(smem + 65536 + (w ^ 4) * 4096);
  v4i r[4];
#pragma unroll
  for (int c = 0; c < 4; ++c) r[c] = (kh ? acc[1][c] : acc[0][c]) + (v4i)Xp[c * 64 + lane];

  const float sx = bf2f(scale_x[0]);
  const float zx = bf16r((float)zero_x[0]);
  const v4i csv = *(const v4i*)&cs_lds[4 * cq];
  const v2u swv = *(const v2u*)&scale_w[n0 + 4 * cq];
  const v2u bv = *(const v2u*)&bias[n0 + 4 * cq];
  float t[4], sw[4], bi[4];
#pragma unroll
  for (int c = 0; c < 4; ++c) {
    t[c] = bf16r(zx * bf16r((float)csv[c]));
    sw[c] = __uint_as_float((c & 1) ? (swv[c >> 1] & 0xffff0000u) : (swv[c >> 1] << 16));
    bi[c] = __uint_as_float((c & 1) ? (bv[c >> 1] & 0xffff0000u) : (bv[c >> 1] << 16));
  }
#pragma unroll
  for (int reg = 0; reg < 4; ++reg) {
    unsigned short o[4];
#pragma unroll
    for (int c = 0; c < 4; ++c) {
      float v = bf16r((float)r[c][reg]);
      v = bf16r(v - t[c]);
      v = bf16r(v * sx);
      v = bf16r(v * sw[c]);
      __bf16 h = (__bf16)(v + bi[c]);
      o[c] = __builtin_bit_cast(unsigned short, h);
    }
    const int row = m0 + 32 * mg + 16 * kh + 4 * q + reg;
    v2u ov = {(unsigned)o[0] | ((unsigned)o[1] << 16), (unsigned)o[2] | ((unsigned)o[3] << 16)};
    *(v2u*)&out[(size_t)row * N + n0 + 4 * cq] = ov;
  }
}

extern "C" void kernel_launch(void* const* d_in, const int* in_sizes, int n_in, void* d_out, int out_size, void* d_ws,
                              size_t ws_size, hipStream_t stream) {
  constexpr int LDS = 65536 + 32768 + 256;
  (void)hipFuncSetAttribute((const void*)w8a8_kernel, hipFuncAttributeMaxDynamicSharedMemorySize, LDS);
  w8a8_kernel<<<224, 512, LDS, stream>>>((const int*)d_in[0], (const int*)d_in[1], (const unsigned short*)d_in[2],
                                         (const int*)d_in[3], (const unsigned short*)d_in[4],
                                         (const unsigned short*)d_in[5], (unsigned short*)d_out);
}
```

```cpp
#include <hip/hip_runtime.h>
#include <hip/hip_bf16.h>
#include <stdint.h>

typedef int v4i __attribute__((ext_vector_type(4)));
typedef unsigned v4u __attribute__((ext_vector_type(4)));
typedef unsigned v2u __attribute__((ext_vector_type(2)));

constexpr int M = 512, K = 1024, N = 3584;

__device__ __forceinline__ unsigned pack4(unsigned a, unsigned b, unsigned c, unsigned d) {
  unsigned t0 = __builtin_amdgcn_perm(b, a, 0x0c0c0400u);
  unsigned t1 = __builtin_amdgcn_perm(d, c, 0x04000c0cu);
  return t0 | t1;
}
__device__ __forceinline__ float bf16r(float x) {
  __bf16 h = (__bf16)x;
  return (float)h;
}
__device__ __forceinline__ float bf2f(unsigned short h) { return __uint_as_float((unsigned)h << 16); }

__global__ __launch_bounds__(512, 2) void w8a8_kernel(const int* __restrict__ qx, const int* __restrict__ qw,
                                                      const unsigned short* __restrict__ scale_x,
                                                      const int* __restrict__ zero_x,
                                                      const unsigned short* __restrict__ scale_w,
                                                      const unsigned short* __restrict__ bias,
                                                      unsigned short* __restrict__ out) {
  const unsigned long long T0 = __builtin_amdgcn_s_memrealtime();
  extern __shared__ __attribute__((aligned(16))) unsigned char smem[];
  v4u* Bs = (v4u*)smem;
  const int tid = threadIdx.x;
  const int lane = tid & 63;
  const int w = __builtin_amdgcn_readfirstlane(tid >> 6);
  v4u* Xs = (v4u*)(smem + 65536 + w * 4096);
  int* cs_lds = (int*)(smem + 65536 + 32768);

  const int b = blockIdx.x;
  const int xcd = b & 7, idx = b >> 3;
  const int ntile = xcd * 7 + (idx >> 2), mq = idx & 3;
  const int n0 = ntile * 64, m0 = mq * 128;

  const __amdgpu_buffer_rsrc_t rw = __builtin_amdgcn_make_buffer_rsrc((void*)qw, 0, K * N * 4, 0x00020000);
  const __amdgpu_buffer_rsrc_t rx = __builtin_amdgcn_make_buffer_rsrc((void*)qx, 0, M * K * 4, 0x00020000);

  if (tid < 64) cs_lds[tid] = 0;

  const int cq = lane & 15, q = lane >> 4;
  const unsigned wvoff = (unsigned)((128 * w + 4 * q) * N + n0 + 4 * cq) * 4u;
  v4u wv[2][16];
#pragma unroll
  for (int g = 0; g < 2; ++g)
#pragma unroll
    for (int j = 0; j < 16; ++j)
      wv[g][j] = __builtin_amdgcn_raw_buffer_load_b128(rw, wvoff, (32 * (j >> 2) + 16 * g + (j & 3)) * N * 4, 0);
  __builtin_amdgcn_sched_barrier(0);

  const int mg = w & 3, kh = w >> 2;
  const int rr = lane >> 3, s = lane & 7;
  const unsigned xvoff = (unsigned)((m0 + 32 * mg + rr) * K + 512 * kh + 4 * s) * 4u;
  v4u xv[2][16];
  int cs[4] = {0, 0, 0, 0};
  __syncthreads();
#pragma unroll
  for (int g = 0; g < 2; ++g) {
#pragma unroll
    for (int c = 0; c < 4; ++c) {
      v4u p;
#pragma unroll
      for (int d = 0; d < 4; ++d) {
        p[d] = pack4(wv[g][4 * d][c], wv[g][4 * d + 1][c], wv[g][4 * d + 2][c], wv[g][4 * d + 3][c]);
        cs[c] = __builtin_amdgcn_sdot4((int)p[d], 0x01010101, cs[c], false);
      }
      Bs[((2 * w + g) * 4 + c) * 64 + lane] = p;
    }
    __builtin_amdgcn_sched_barrier(0);
#pragma unroll
    for (int rg = 0; rg < 4; ++rg)
#pragma unroll
      for (int i = 0; i < 4; ++i)
        xv[g][rg * 4 + i] = __builtin_amdgcn_raw_buffer_load_b128(rx, xvoff + i * 128, (rg * 8 * K + g * 128) * 4, 0);
    __builtin_amdgcn_sched_barrier(0);
  }
#pragma unroll
  for (int c = 0; c < 4; ++c) atomicAdd(&cs_lds[4 * cq + c], cs[c]);
  __builtin_amdgcn_s_waitcnt(0xC07F);
  __builtin_amdgcn_s_barrier();
  __builtin_amdgcn_sched_barrier(0);

  v4i acc[2][4];
#pragma unroll
  for (int mt = 0; mt < 2; ++mt)
#pragma unroll
    for (int c = 0; c < 4; ++c) acc[mt][c] = (v4i){0, 0, 0, 0};

  const int r16 = lane & 15;
#pragma unroll
  for (int ch = 0; ch < 4; ++ch) {
    const int bu = ch & 1;
#pragma unroll
    for (int rg = 0; rg < 4; ++rg) {
      v4u p;
#pragma unroll
      for (int i = 0; i < 4; ++i) {
        v4u v = xv[bu][rg * 4 + i];
        p[i] = pack4(v[0], v[1], v[2], v[3]);
      }
      const int row = 8 * (rg & 1) + rr;
      Xs[((rg >> 1) * 2 + (s >> 2)) * 64 + 16 * (s & 3) + (row ^ s)] = p;
    }
    __builtin_amdgcn_sched_barrier(0);
    if (ch + 2 < 4) {
#pragma unroll
      for (int rg = 0; rg < 4; ++rg)
#pragma unroll
        for (int i = 0; i < 4; ++i)
          xv[bu][rg * 4 + i] =
              __builtin_amdgcn_raw_buffer_load_b128(rx, xvoff + i * 128, (rg * 8 * K + (ch + 2) * 128) * 4, 0);
    }
    __builtin_amdgcn_sched_barrier(0);
#pragma unroll
    for (int t = 0; t < 2; ++t) {
      const int kstep = (kh * 4 + ch) * 2 + t;
      v4u a[2], bb[4];
#pragma unroll
      for (int mt = 0; mt < 2; ++mt) a[mt] = Xs[(mt * 2 + t) * 64 + 16 * q + (r16 ^ (4 * t + q))];
#pragma unroll
      for (int c = 0; c < 4; ++c) bb[c] = Bs[(kstep * 4 + c) * 64 + lane];
#pragma unroll
      for (int mt = 0; mt < 2; ++mt)
#pragma unroll
        for (int c = 0; c < 4; ++c)
          acc[mt][c] = __builtin_amdgcn_mfma_i32_16x16x64_i8((v4i)a[mt], (v4i)bb[c], acc[mt][c], 0, 0, 0);
    }
    __builtin_amdgcn_sched_barrier(0);
  }

#pragma unroll
  for (int c = 0; c < 4; ++c) Xs[c * 64 + lane] = (v4u)(kh ? acc[0][c] : acc[1][c]);
  __syncthreads();
  const v4u* Xp = (const v4u*)(smem + 65536 + (w ^ 4) * 4096);
  v4i r[4];
#pragma unroll
  for (int c = 0; c < 4; ++c) r[c] = (kh ? acc[1][c] : acc[0][c]) + (v4i)Xp[c * 64 + lane];

  const float sx = bf2f(scale_x[0]);
  const float zx = bf16r((float)zero_x[0]);
  const v4i csv = *(const v4i*)&cs_lds[4 * cq];
  const v2u swv = *(const v2u*)&scale_w[n0 + 4 * cq];
  const v2u bv = *(const v2u*)&bias[n0 + 4 * cq];
  float t[4], sw[4], bi[4];
#pragma unroll
  for (int c = 0; c < 4; ++c) {
    t[c] = bf16r(zx * bf16r((float)csv[c]));
    sw[c] = __uint_as_float((c & 1) ? (swv[c >> 1] & 0xffff0000u) : (swv[c >> 1] << 16));
    bi[c] = __uint_as_float((c & 1) ? (bv[c >> 1] & 0xffff0000u) : (bv[c >> 1] << 16));
  }
#pragma unroll
  for (int reg = 0; reg < 4; ++reg) {
    unsigned short o[4];
#pragma unroll
    for (int c = 0; c < 4; ++c) {
      float v = bf16r((float)r[c][reg]);
      v = bf16r(v - t[c]);
      v = bf16r(v * sx);
      v = bf16r(v * sw[c]);
      __bf16 h = (__bf16)(v + bi[c]);
      o[c] = __builtin_bit_cast(unsigned short, h);
    }
    const int row = m0 + 32 * mg + 16 * kh + 4 * q + reg;
    v2u ov = {(unsigned)o[0] | ((unsigned)o[1] << 16), (unsigned)o[2] | ((unsigned)o[3] << 16)};
    *(v2u*)&out[(size_t)row * N + n0 + 4 * cq] = ov;
  }
  asm volatile("s_waitcnt vmcnt(0)" ::: "memory");
  __builtin_amdgcn_sched_barrier(0);
  if (b == 0) while (__builtin_amdgcn_s_memrealtime() - T0 < 4000ull) __builtin_amdgcn_s_sleep(1);
  __builtin_amdgcn_sched_barrier(0);
}

extern "C" void kernel_launch(void* const* d_in, const int* in_sizes, int n_in, void* d_out, int out_size, void* d_ws,
                              size_t ws_size, hipStream_t stream) {
  constexpr int LDS = 65536 + 32768 + 256;
  (void)hipFuncSetAttribute((const void*)w8a8_kernel, hipFuncAttributeMaxDynamicSharedMemorySize, LDS);
  w8a8_kernel<<<224, 512, LDS, stream>>>((const int*)d_in[0], (const int*)d_in[1], (const unsigned short*)d_in[2],
                                         (const int*)d_in[3], (const unsigned short*)d_in[4],
                                         (const unsigned short*)d_in[5], (unsigned short*)d_out);
}
```

```cpp
#include <hip/hip_runtime.h>
#include <stdint.h>

typedef int v4i __attribute__((ext_vector_type(4)));
typedef unsigned v4u __attribute__((ext_vector_type(4)));
typedef unsigned v2u __attribute__((ext_vector_type(2)));
typedef float v2f __attribute__((ext_vector_type(2)));
typedef __bf16 v2b __attribute__((ext_vector_type(2)));
typedef unsigned long long u64;
#define GAS __attribute__((address_space(1)))
#define LAS __attribute__((address_space(3)))
#define RLX_AGENT __ATOMIC_RELAXED, __HIP_MEMORY_SCOPE_AGENT
#define SCHED() __builtin_amdgcn_sched_barrier(0)

constexpr int M = 512, K = 1024, N = 3584;
constexpr int NPACK = 32, NWG = 256, NTHR = 512;
constexpr int WS_X8 = 0;
constexpr int WS_FLAG = 512 * 1024;
constexpr int WS_EPOCH = WS_FLAG + 256 * 128;
constexpr int WS_BYTES = 576 * 1024;
constexpr int LDS_B = 0;
constexpr int LDS_CS = 65536;
constexpr int LDS_GO = LDS_CS + 256;
constexpr int LDS_BYTES = 84 * 1024;
#define AUX_OUT 17
#define POLL_MAXWAIT 600
#define LAG 3
#define WWIN 12
#define WWIN2 12
#define WSW 9
#define PSLEEP 4
#define GO_GROUP 11
#define XWIN 3
#define XGO 12
#define XPF 0
#define TLBW 0
#define FASTEPI 0
constexpr int BD = 2;
constexpr int QF = 2;

#define STAMP(i)
#define STAMP_OUT(grp)
#define STAMP_ARG
#define SPAN_OUT()

__device__ __forceinline__ unsigned pack4(unsigned a, unsigned b, unsigned c, unsigned d) {
  return __builtin_amdgcn_perm(b, a, 0x0c0c0400u) | __builtin_amdgcn_perm(d, c, 0x04000c0cu);
}
__device__ __forceinline__ unsigned cvt_pk_bf16(float a, float b) {
  v2f f = {a, b};
  return __builtin_bit_cast(unsigned, __builtin_convertvector(f, v2b));
}
__device__ __forceinline__ float bf16r(float x) { return __uint_as_float(cvt_pk_bf16(x, x) << 16); }
__device__ __forceinline__ void bar() {
  __builtin_amdgcn_s_barrier();
  SCHED();
}
__device__ __forceinline__ void wait_flags(GAS u64* mine, bool sel, u64 tag) {
#pragma unroll 1
  for (unsigned spins = 0; spins < (1u << 18); ++spins) {
    if (sel) sel = __hip_atomic_load(mine, RLX_AGENT) != tag;
    if (!__any(sel)) break;
    __builtin_amdgcn_s_sleep(PSLEEP);
  }
  __builtin_amdgcn_fence(__ATOMIC_ACQUIRE, "wavefront");
}

__global__ __launch_bounds__(NTHR, 2) void w8a8_kernel(const int* __restrict__ qx, const int* __restrict__ qw,
                                                       unsigned char* ws, unsigned short* __restrict__ out,
                                                       const unsigned short* __restrict__ scale_x,
                                                       const int* __restrict__ zero_x,
                                                       const unsigned short* __restrict__ scale_w,
                                                       const unsigned short* __restrict__ bias STAMP_ARG) {
  extern __shared__ __attribute__((aligned(16))) unsigned char smem[];
  const u64 t_start = __builtin_amdgcn_s_memrealtime();
  const int tid = threadIdx.x;
  const int lane = tid & 63;
  const int w = __builtin_amdgcn_readfirstlane(tid >> 6);
  const int b = blockIdx.x;
  const int xcd = b & 7, idx = b >> 3;
  const int mq = idx & 3, grp = idx >> 2;
  const int m0 = mq * 128;
  const int l15 = lane & 15, q = lane >> 4;
  const bool cons = b >= NPACK;
  const int n0 = (xcd * 7 + grp - 1) * 64;
  const int j = grp * 8 + xcd;
  asm volatile("" ::"s"(qx), "s"(qw), "s"(ws), "s"(out));
  const __amdgpu_buffer_rsrc_t rs = __builtin_amdgcn_make_buffer_rsrc((void*)(ws + WS_X8), 0, M * K, 0x00020000);
  const __amdgpu_buffer_rsrc_t rw = __builtin_amdgcn_make_buffer_rsrc((void*)qw, 0, K * N * 4, 0x00020000);
  const __amdgpu_buffer_rsrc_t rx = __builtin_amdgcn_make_buffer_rsrc((void*)qx, 0, M * K * 4, 0x00020000);
  const __amdgpu_buffer_rsrc_t ro = __builtin_amdgcn_make_buffer_rsrc((void*)out, 0, M * N * 2, 0x00020000);
  GAS u64* epoch = (GAS u64*)(ws + WS_EPOCH);
  GAS u64* flags = (GAS u64*)(ws + WS_FLAG);
  volatile LAS unsigned* sync = (volatile LAS unsigned*)(smem + LDS_GO);
  const int w3 = w & 3;
  const bool loader = cons && w >= 4;
  const unsigned wl = (unsigned)((64 * w3 + 16 * q) * N + n0 + 4 * l15) * 4u;
  const int lcls = ((int)((uintptr_t)qw >> 7) + (n0 >> 5)) & 7;
  const int q0 = (lcls == 2 || lcls == 3) ? mq : 2 * (mq & 1);

  unsigned warm = 0;
  if (XPF && cons && w == 1) {
    warm = __builtin_amdgcn_raw_buffer_load_b32(rx, (unsigned)((m0 + 16 * xcd + (lane >> 2)) * K + 128 * grp + 32 * (lane & 3)) * 4u, 0, 0);
    asm volatile("" ::: "memory");
  }
  v4u raw[LAG + 1][4];
  if (loader) {
#pragma unroll
    for (int e = 0; e < WWIN; ++e) raw[e >> 2][e & 3] = __builtin_amdgcn_raw_buffer_load_b128(rw, wl + q0 * (256 * N * 4), e * N * 4, 0);
    asm volatile("" ::: "memory");
  }

  if (!cons) {
    const unsigned xoff = (unsigned)((m0 + 16 * xcd + (lane >> 5)) * K + 128 * w + 4 * (lane & 31)) * 4u;
    v4u xv[8];
#pragma unroll
    for (int i = 0; i < 8; ++i) xv[i] = __builtin_amdgcn_raw_buffer_load_b128(rx, xoff, i * 2 * K * 4, 0);
    SCHED();
    u64 tag = 0;
    if (w == 0 || (w == 1 && b == 0)) tag = __hip_atomic_load(epoch, RLX_AGENT) + 1ull;
    unsigned* xt = (unsigned*)(smem + w * 2304);
#pragma unroll
    for (int i = 0; i < 8; ++i) xt[(2 * i + (lane >> 5)) * 36 + (lane & 31)] = pack4(xv[i][0], xv[i][1], xv[i][2], xv[i][3]);
#pragma unroll
    for (int f = 0; f < 2; ++f) {
      const v4u p = *(const v4u*)(xt + l15 * 36 + 16 * f + 4 * q);
      __builtin_amdgcn_raw_buffer_store_b128(p, rs, (unsigned)(((mq * 8 + j) * 16 + 2 * w + f) * 1024 + lane * 16), 0, 16);
    }
    STAMP(1);
    asm volatile("s_waitcnt vmcnt(0)" ::: "memory");
    bar();
    STAMP(2);
    if (w == 0 && lane == 0) __hip_atomic_store(flags + (mq * 64 + j) * 16, tag, RLX_AGENT);
    if (w == 1 && b == 0) {
#pragma unroll 1
      for (int r = 0; r < 4; ++r) wait_flags(flags + (r * 64 + lane) * 16, true, tag);
      if (lane == 0) __hip_atomic_store(epoch, tag, RLX_AGENT);
    }
    if (w == 7) STAMP_OUT(0);
    SPAN_OUT();
    return;
  }

  if (w == 2) {
    if (lane < 8) sync[lane] = 0u;
    ((int*)(smem + LDS_CS))[lane] = 0;
    __builtin_amdgcn_s_waitcnt(0xC07F);
  }
  bar();

  if (loader) {
    v4u pk[4];
    int cs[4] = {0, 0, 0, 0};
    v4u* dst = (v4u*)(smem + LDS_B) + w3 * 256 + lane;
#pragma unroll
    for (int g = 0; g < 16 + LAG; ++g) {
      if (g < 16) {
        const unsigned off = wl + ((q0 + (g >> 2)) & 3) * (256 * N * 4);
#pragma unroll
        for (int e = 0; e < 4; ++e) {
          if (4 * g + e < WWIN) continue;
          if (g >= WSW) asm volatile("s_waitcnt vmcnt(%0)" ::"n"(WWIN2 - 1) : "memory");
          else asm volatile("s_waitcnt vmcnt(%0)" ::"n"(WWIN - 1) : "memory");
          raw[g % (LAG + 1)][e] = __builtin_amdgcn_raw_buffer_load_b128(rw, off, (4 * (g & 3) + e) * N * 4, 0);
          SCHED();
        }
      }
      if (g == 3) { STAMP(1); }
      if (g == 7) { STAMP(2); }
      if (g == GO_GROUP) sync[4] = 1u;
      if (g == XGO) sync[6] = 1u;
      if (g == 11) { STAMP(3); }
      if (g == 15) { STAMP(4); }
      if (g >= LAG) {
        const int p = g - LAG;
#pragma unroll
        for (int c = 0; c < 4; ++c) {
          pk[c][p & 3] = pack4(raw[p % (LAG + 1)][0][c], raw[p % (LAG + 1)][1][c], raw[p % (LAG + 1)][2][c], raw[p % (LAG + 1)][3][c]);
          cs[c] = __builtin_amdgcn_sdot4((int)pk[c][p & 3], 0x01010101, cs[c], false);
        }
        if ((p & 3) == 3) {
          v4u* d2 = dst + ((q0 + (p >> 2)) & 3) * 1024;
#pragma unroll
          for (int c = 0; c < 4; ++c) d2[c * 64] = pk[c];
          if (p == 15) {
#pragma unroll
            for (int c = 0; c < 4; ++c) atomicAdd((int*)(smem + LDS_CS) + 4 * l15 + c, cs[c]);
          }
          asm volatile("s_waitcnt lgkmcnt(0)" ::: "memory");
          sync[w3] = (unsigned)(p >> 2) + 1u;
        }
        SCHED();
      }
    }
    STAMP(5);
    if (w == 4) STAMP_OUT(1);
    return;
  }

  u64 tag = 0;
  if (w == 0) tag = __hip_atomic_load(epoch, RLX_AGENT) + 1ull;
  const unsigned ooff = (unsigned)((m0 + 32 * w + 4 * q) * N + n0 + 4 * l15) * 2u;
  if (TLBW && w >= 2) warm = __builtin_amdgcn_raw_buffer_load_b32(ro, (unsigned)((m0 + 127 * (w - 2)) * N + n0) * 2u, 0, 0);
  const v2u swv = *(const v2u*)&scale_w[n0 + 4 * l15];
  const v2u bv = *(const v2u*)&bias[n0 + 4 * l15];
  const unsigned sxraw = scale_x[0];
  const int zxraw = zero_x[0];
  unsigned xs[16], bs[16];
  {
    const int rot = (4 * QF * (grp - 1)) / 7;
#pragma unroll
    for (int t = 0; t < 16; ++t) {
      const int ks = (4 * q0 + (t < 4 * QF ? (t + rot) & (4 * QF - 1) : t)) & 15;
      xs[t] = (unsigned)(ks * 1024);
      bs[t] = (unsigned)(uintptr_t)((LAS unsigned char*)smem + LDS_B + ks * 4096 + lane * 16);
      asm volatile("" : "+s"(xs[t]), "+v"(bs[t]));
    }
  }
  SCHED();
  if (w == 0) {
    if (lane == 0) __hip_atomic_store(flags + (mq * 64 + j) * 16, tag, RLX_AGENT);
    STAMP(1);
    while (sync[4] != 1u && __builtin_amdgcn_s_memrealtime() - t_start < POLL_MAXWAIT) __builtin_amdgcn_s_sleep(2);
    STAMP(2);
    wait_flags(flags + (mq * 64 + lane) * 16, lane < 8, tag);
    sync[5] = 1u;
    STAMP(3);
  }
  asm volatile("" ::"v"(warm));
  while (sync[5] + sync[6] != 2u) __builtin_amdgcn_s_sleep(1);
  asm volatile("" ::: "memory");
  if (w == 3) STAMP(1);
  const unsigned x8off = (unsigned)((mq * 8 + 2 * w) * 16384 + lane * 16);
  v4u xa[16][2];
#define XLOAD(t) do { _Pragma("unroll") for (int mt = 0; mt < 2; ++mt) xa[t][mt] = __builtin_amdgcn_raw_buffer_load_b128(rs, x8off + mt * 16384, xs[t], 16); } while (0)
#pragma unroll
  for (int t = 0; t < XWIN; ++t) XLOAD(t);
  SCHED();
  v4i acc[2][4];
#pragma unroll
  for (int mt = 0; mt < 2; ++mt)
#pragma unroll
    for (int c = 0; c < 4; ++c) acc[mt][c] = (v4i){0, 0, 0, 0};
  v4u bb[BD + 1][4];
  v4i csv;
  float sv[4], ov[4];
#define BLOAD(t) do { _Pragma("unroll") for (int c = 0; c < 4; ++c) bb[(t) % (BD + 1)][c] = *(const LAS v4u*)(uintptr_t)(bs[t] + c * 1024); } while (0)
#define WAITQ(need) do { for (;;) { const v4u pr = *(volatile LAS v4u*)sync; \
      if (__builtin_amdgcn_readfirstlane(min(min(pr[0], pr[1]), min(pr[2], pr[3]))) >= (need)) break; __builtin_amdgcn_s_sleep(1); } \
    asm volatile("" ::: "memory"); } while (0)
  WAITQ((unsigned)QF);
  v4u prog;
  if (w == 0) STAMP(4);
  BLOAD(0);
  BLOAD(1);
  SCHED();
#pragma unroll
  for (int t = 0; t < 16; ++t) {
    if (t + BD < 16 && ((t + BD) & 3) == 0 && (t + BD) / 4 >= QF) {
      const unsigned need = (unsigned)((t + BD) / 4) + 1u;
      if (__builtin_amdgcn_readfirstlane(min(min(prog[0], prog[1]), min(prog[2], prog[3]))) < need) WAITQ(need);
      asm volatile("" ::: "memory");
      if (need == 4u) csv = *(const v4i*)(smem + LDS_CS + 16 * l15);
      SCHED();
    }
    if (t + 1 + BD < 16 && ((t + 1 + BD) & 3) == 0 && (t + 1 + BD) / 4 >= QF) { prog = *(volatile LAS v4u*)sync; SCHED(); }
    if (w == 3) { if (t == 1) STAMP(2); if (t == 8) STAMP(3); if (t == 12) STAMP(4); }
    if (t + BD < 16) BLOAD(t + BD);
    if (t + XWIN < 16) XLOAD(t + XWIN);
#pragma unroll
    for (int mt = 0; mt < 2; ++mt)
#pragma unroll
      for (int c = 0; c < 4; ++c)
        acc[mt][c] = __builtin_amdgcn_mfma_i32_16x16x64_i8((v4i)xa[t][mt], (v4i)bb[t % (BD + 1)][c], acc[mt][c], 0, 0, 0);
    if (t >= 12) {
      const int c = t - 12;
      const float tz = bf16r(bf16r((float)zxraw) * bf16r((float)csv[c]));
      sv[c] = __uint_as_float(sxraw << 16) * __uint_as_float((c & 1) ? (swv[c >> 1] & 0xffff0000u) : (swv[c >> 1] << 16));
      ov[c] = __builtin_fmaf(-tz, sv[c], __uint_as_float((c & 1) ? (bv[c >> 1] & 0xffff0000u) : (bv[c >> 1] << 16)));
    }
#pragma unroll
    for (int i = 0; i < 8; ++i) {
      __builtin_amdgcn_sched_group_barrier(0x008, 1, 0);
      if (i < 4) __builtin_amdgcn_sched_group_barrier(0x100, 1, 0);
      else if (i < 6) __builtin_amdgcn_sched_group_barrier(0x020, 1, 0);
      __builtin_amdgcn_sched_group_barrier(0x002, 2, 0);
    }
    SCHED();
  }
  STAMP(5);
#pragma unroll
  for (int mt = 0; mt < 2; ++mt)
#pragma unroll
    for (int reg = 0; reg < 4; ++reg) {
      v2u o;
#pragma unroll
      for (int cp = 0; cp < 2; ++cp) {
        v2f a = {(float)acc[mt][2 * cp][reg], (float)acc[mt][2 * cp + 1][reg]};
        if (!FASTEPI) {
          const unsigned pk = cvt_pk_bf16(a[0], a[1]);
          a = (v2f){__uint_as_float(pk << 16), __uint_as_float(pk & 0xffff0000u)};
        }
        const v2f s2 = {sv[2 * cp], sv[2 * cp + 1]}, o2 = {ov[2 * cp], ov[2 * cp + 1]};
        const v2f r = __builtin_elementwise_fma(a, s2, o2);
        o[cp] = cvt_pk_bf16(r[0], r[1]);
      }
      __builtin_amdgcn_raw_buffer_store_b64(o, ro, ooff, (mt * 16 + reg) * N * 2, AUX_OUT);
    }
  SPAN_OUT();
}

extern "C" void kernel_launch(void* const* d_in, const int* in_sizes, int n_in, void* d_out, int out_size, void* d_ws,
                              size_t ws_size, hipStream_t stream) {
  (void)hipFuncSetAttribute((const void*)w8a8_kernel, hipFuncAttributeMaxDynamicSharedMemorySize, LDS_BYTES);
  unsigned char* ws = (unsigned char*)d_ws + ((ws_size - WS_BYTES) & ~(size_t)4095);
  w8a8_kernel<<<NWG, NTHR, LDS_BYTES, stream>>>((const int*)d_in[0], (const int*)d_in[1], ws, (unsigned short*)d_out,
                                                (const unsigned short*)d_in[2], (const int*)d_in[3],
                                                (const unsigned short*)d_in[4], (const unsigned short*)d_in[5]
  );
}
```

```cpp
#include <hip/hip_runtime.h>
#include <stdint.h>

typedef int v4i __attribute__((ext_vector_type(4)));
typedef unsigned v4u __attribute__((ext_vector_type(4)));
typedef unsigned v2u __attribute__((ext_vector_type(2)));
typedef float v2f __attribute__((ext_vector_type(2)));
typedef __bf16 v2b __attribute__((ext_vector_type(2)));
typedef unsigned long long u64;
#define GAS __attribute__((address_space(1)))
#define LAS __attribute__((address_space(3)))
#define RLX_AGENT __ATOMIC_RELAXED, __HIP_MEMORY_SCOPE_AGENT
#define SCHED() __builtin_amdgcn_sched_barrier(0)

constexpr int M = 512, K = 1024, N = 3584;
constexpr int NPACK = 32, NWG = 256, NTHR = 512;
constexpr int WS_X8 = 0;
constexpr int WS_FLAG = 512 * 1024;
constexpr int WS_EPOCH = WS_FLAG + 256 * 128;
constexpr int WS_BYTES = 576 * 1024;
constexpr int LDS_B = 0;
constexpr int LDS_CS = 65536;
constexpr int LDS_GO = LDS_CS + 256;
constexpr int LDS_BYTES = 84 * 1024;
constexpr int AUX_DEV = 16;
constexpr int AUX_OUT = 17;
constexpr int LAG = 3;
constexpr int WWIN = 12;
constexpr int POLL_GROUP = 11;
constexpr int XGO_GROUP = 12;
constexpr int XWIN = 3;
constexpr int BD = 2;

__device__ __forceinline__ unsigned pack4(unsigned a, unsigned b, unsigned c, unsigned d) {
  return __builtin_amdgcn_perm(b, a, 0x0c0c0400u) | __builtin_amdgcn_perm(d, c, 0x04000c0cu);
}
__device__ __forceinline__ unsigned cvt_pk_bf16(float a, float b) {
  v2f f = {a, b};
  return __builtin_bit_cast(unsigned, __builtin_convertvector(f, v2b));
}
__device__ __forceinline__ float bf16r(float x) { return __uint_as_float(cvt_pk_bf16(x, x) << 16); }
__device__ __forceinline__ void bar() {
  __builtin_amdgcn_s_barrier();
  SCHED();
}
__device__ __forceinline__ void wait_flags(GAS u64* mine, bool sel, u64 tag) {
#pragma unroll 1
  for (unsigned spins = 0; spins < (1u << 18); ++spins) {
    if (sel) sel = __hip_atomic_load(mine, RLX_AGENT) != tag;
    if (!__any(sel)) break;
    __builtin_amdgcn_s_sleep(4);
  }
  __builtin_amdgcn_fence(__ATOMIC_ACQUIRE, "wavefront");
}
__device__ __forceinline__ unsigned min4(v4u p) {
  return __builtin_amdgcn_readfirstlane(min(min(p[0], p[1]), min(p[2], p[3])));
}
__device__ __forceinline__ void wait_quarters(volatile LAS unsigned* sync, unsigned need) {
  while (min4(*(volatile LAS v4u*)sync) < need) __builtin_amdgcn_s_sleep(1);
  asm volatile("" ::: "memory");
}

__global__ __launch_bounds__(NTHR, 2) void w8a8_kernel(const int* __restrict__ qx, const int* __restrict__ qw,
                                                       unsigned char* ws, unsigned short* __restrict__ out,
                                                       const unsigned short* __restrict__ scale_x,
                                                       const int* __restrict__ zero_x,
                                                       const unsigned short* __restrict__ scale_w,
                                                       const unsigned short* __restrict__ bias) {
  extern __shared__ __attribute__((aligned(16))) unsigned char smem[];
  const int tid = threadIdx.x;
  const int lane = tid & 63;
  const int w = __builtin_amdgcn_readfirstlane(tid >> 6);
  const int b = blockIdx.x;
  const int xcd = b & 7, idx = b >> 3;
  const int mq = idx & 3, grp = idx >> 2;
  const int m0 = mq * 128;
  const int l15 = lane & 15, q = lane >> 4;
  const bool cons = b >= NPACK;
  const int n0 = (xcd * 7 + grp - 1) * 64;
  const int j = grp * 8 + xcd;
  asm volatile("" ::"s"(qx), "s"(qw), "s"(ws), "s"(out));
  const __amdgpu_buffer_rsrc_t rs = __builtin_amdgcn_make_buffer_rsrc((void*)(ws + WS_X8), 0, M * K, 0x00020000);
  const __amdgpu_buffer_rsrc_t rw = __builtin_amdgcn_make_buffer_rsrc((void*)qw, 0, K * N * 4, 0x00020000);
  GAS u64* epoch = (GAS u64*)(ws + WS_EPOCH);
  GAS u64* flags = (GAS u64*)(ws + WS_FLAG);
  volatile LAS unsigned* sync = (volatile LAS unsigned*)(smem + LDS_GO);
  const int w3 = w & 3;
  const bool loader = cons && w >= 4;
  const unsigned wl = (unsigned)((64 * w3 + 16 * q) * N + n0 + 4 * l15) * 4u;
  const int lcls = ((int)((uintptr_t)qw >> 7) + (n0 >> 5)) & 7;
  const int q0 = (lcls == 2 || lcls == 3) ? mq : 2 * (mq & 1);

  v4u raw[LAG + 1][4];
  if (loader) {
#pragma unroll
    for (int e = 0; e < WWIN; ++e)
      raw[e >> 2][e & 3] = __builtin_amdgcn_raw_buffer_load_b128(rw, wl + q0 * (256 * N * 4), e * N * 4, 0);
    asm volatile("" ::: "memory");
  }

  if (!cons) {
    const __amdgpu_buffer_rsrc_t rx = __builtin_amdgcn_make_buffer_rsrc((void*)qx, 0, M * K * 4, 0x00020000);
    const unsigned xoff = (unsigned)((m0 + 16 * xcd + (lane >> 5)) * K + 128 * w + 4 * (lane & 31)) * 4u;
    v4u xv[8];
#pragma unroll
    for (int i = 0; i < 8; ++i) xv[i] = __builtin_amdgcn_raw_buffer_load_b128(rx, xoff, i * 2 * K * 4, 0);
    SCHED();
    u64 tag = 0;
    if (w == 0 || (w == 1 && b == 0)) tag = __hip_atomic_load(epoch, RLX_AGENT) + 1ull;
    unsigned* xt = (unsigned*)(smem + w * 2304);
#pragma unroll
    for (int i = 0; i < 8; ++i)
      xt[(2 * i + (lane >> 5)) * 36 + (lane & 31)] = pack4(xv[i][0], xv[i][1], xv[i][2], xv[i][3]);
#pragma unroll
    for (int f = 0; f < 2; ++f) {
      const v4u p = *(const v4u*)(xt + l15 * 36 + 16 * f + 4 * q);
      __builtin_amdgcn_raw_buffer_store_b128(p, rs, (unsigned)(((mq * 8 + j) * 16 + 2 * w + f) * 1024 + lane * 16), 0,
                                             AUX_DEV);
    }
    asm volatile("s_waitcnt vmcnt(0)" ::: "memory");
    bar();
    if (w == 0 && lane == 0) __hip_atomic_store(flags + (mq * 64 + j) * 16, tag, RLX_AGENT);
    if (w == 1 && b == 0) {
#pragma unroll 1
      for (int r = 0; r < 4; ++r) wait_flags(flags + (r * 64 + lane) * 16, true, tag);
      if (lane == 0) __hip_atomic_store(epoch, tag, RLX_AGENT);
    }
    return;
  }

  if (w == 2) {
    if (lane < 8) sync[lane] = 0u;
    ((int*)(smem + LDS_CS))[lane] = 0;
    __builtin_amdgcn_s_waitcnt(0xC07F);
  }
  bar();

  if (loader) {
    v4u pk[4];
    int cs[4] = {0, 0, 0, 0};
    v4u* dst = (v4u*)(smem + LDS_B) + w3 * 256 + lane;
#pragma unroll
    for (int g = 0; g < 16 + LAG; ++g) {
      if (g < 16) {
        const unsigned off = wl + ((q0 + (g >> 2)) & 3) * (256 * N * 4);
#pragma unroll
        for (int e = 0; e < 4; ++e) {
          if (4 * g + e < WWIN) continue;
          asm volatile("s_waitcnt vmcnt(%0)" ::"n"(WWIN - 1) : "memory");
          raw[g % (LAG + 1)][e] = __builtin_amdgcn_raw_buffer_load_b128(rw, off, (4 * (g & 3) + e) * N * 4, 0);
          SCHED();
        }
      }
      if (g == POLL_GROUP) sync[4] = 1u;
      if (g == XGO_GROUP) sync[6] = 1u;
      if (g >= LAG) {
        const int p = g - LAG;
#pragma unroll
        for (int c = 0; c < 4; ++c) {
          pk[c][p & 3] = pack4(raw[p % (LAG + 1)][0][c], raw[p % (LAG + 1)][1][c], raw[p % (LAG + 1)][2][c],
                               raw[p % (LAG + 1)][3][c]);
          cs[c] = __builtin_amdgcn_sdot4((int)pk[c][p & 3], 0x01010101, cs[c], false);
        }
        if ((p & 3) == 3) {
          v4u* d2 = dst + ((q0 + (p >> 2)) & 3) * 1024;
#pragma unroll
          for (int c = 0; c < 4; ++c) d2[c * 64] = pk[c];
          if (p == 15) {
#pragma unroll
            for (int c = 0; c < 4; ++c) atomicAdd((int*)(smem + LDS_CS) + 4 * l15 + c, cs[c]);
          }
          asm volatile("s_waitcnt lgkmcnt(0)" ::: "memory");
          sync[w3] = (unsigned)(p >> 2) + 1u;
        }
        SCHED();
      }
    }
    return;
  }

  u64 tag = 0;
  if (w == 0) tag = __hip_atomic_load(epoch, RLX_AGENT) + 1ull;
  const v2u swv = *(const v2u*)&scale_w[n0 + 4 * l15];
  const v2u bv = *(const v2u*)&bias[n0 + 4 * l15];
  const unsigned sxraw = scale_x[0];
  const int zxraw = zero_x[0];
  unsigned xs[16], bs[16];
  {
    const int rot = (8 * (grp - 1)) / 7;
#pragma unroll
    for (int t = 0; t < 16; ++t) {
      const int ks = (4 * q0 + (t < 8 ? (t + rot) & 7 : t)) & 15;
      xs[t] = (unsigned)(ks * 1024);
      bs[t] = (unsigned)(uintptr_t)((LAS unsigned char*)smem + LDS_B + ks * 4096 + lane * 16);
      asm volatile("" : "+s"(xs[t]), "+v"(bs[t]));
    }
  }
  SCHED();
  if (w == 0) {
    if (lane == 0) __hip_atomic_store(flags + (mq * 64 + j) * 16, tag, RLX_AGENT);
    while (sync[4] != 1u) __builtin_amdgcn_s_sleep(2);
    wait_flags(flags + (mq * 64 + lane) * 16, lane < 8, tag);
    sync[5] = 1u;
  }
  while (sync[5] + sync[6] != 2u) __builtin_amdgcn_s_sleep(1);
  asm volatile("" ::: "memory");
  const unsigned x8off = (unsigned)((mq * 8 + 2 * w) * 16384 + lane * 16);
  v4u xa[16][2];
#define XLOAD(t)                                                                                      \
  do {                                                                                                \
    _Pragma("unroll") for (int mt = 0; mt < 2; ++mt) xa[t][mt] =                                      \
        __builtin_amdgcn_raw_buffer_load_b128(rs, x8off + mt * 16384, xs[t], AUX_DEV);                \
  } while (0)
#pragma unroll
  for (int t = 0; t < XWIN; ++t) XLOAD(t);
  SCHED();
  v4i acc[2][4];
#pragma unroll
  for (int mt = 0; mt < 2; ++mt)
#pragma unroll
    for (int c = 0; c < 4; ++c) acc[mt][c] = (v4i){0, 0, 0, 0};
  v4u bb[BD + 1][4];
  v4i csv;
  float sv[4], ov[4];
#define BLOAD(t)                                                                                      \
  do {                                                                                                \
    _Pragma("unroll") for (int c = 0; c < 4; ++c) bb[(t) % (BD + 1)][c] =                             \
        *(const LAS v4u*)(uintptr_t)(bs[t] + c * 1024);                                               \
  } while (0)
  wait_quarters(sync, 2u);
  v4u prog;
  BLOAD(0);
  BLOAD(1);
  SCHED();
#pragma unroll
  for (int t = 0; t < 16; ++t) {
    if (t + BD == 8 || t + BD == 12) {
      const unsigned need = (unsigned)((t + BD) / 4) + 1u;
      if (min4(prog) < need) wait_quarters(sync, need);
      asm volatile("" ::: "memory");
      if (need == 4u) csv = *(const v4i*)(smem + LDS_CS + 16 * l15);
      SCHED();
    }
    if (t + 1 + BD == 8 || t + 1 + BD == 12) {
      prog = *(volatile LAS v4u*)sync;
      SCHED();
    }
    if (t + BD < 16) BLOAD(t + BD);
    if (t + XWIN < 16) XLOAD(t + XWIN);
#pragma unroll
    for (int mt = 0; mt < 2; ++mt)
#pragma unroll
      for (int c = 0; c < 4; ++c)
        acc[mt][c] = __builtin_amdgcn_mfma_i32_16x16x64_i8((v4i)xa[t][mt], (v4i)bb[t % (BD + 1)][c], acc[mt][c], 0, 0, 0);
    if (t >= 12) {
      const int c = t - 12;
      const float tz = bf16r(bf16r((float)zxraw) * bf16r((float)csv[c]));
      sv[c] = __uint_as_float(sxraw << 16) * __uint_as_float((c & 1) ? (swv[c >> 1] & 0xffff0000u) : (swv[c >> 1] << 16));
      ov[c] = __builtin_fmaf(-tz, sv[c], __uint_as_float((c & 1) ? (bv[c >> 1] & 0xffff0000u) : (bv[c >> 1] << 16)));
    }
#pragma unroll
    for (int i = 0; i < 8; ++i) {
      __builtin_amdgcn_sched_group_barrier(0x008, 1, 0);
      if (i < 4) __builtin_amdgcn_sched_group_barrier(0x100, 1, 0);
      else if (i < 6) __builtin_amdgcn_sched_group_barrier(0x020, 1, 0);
      __builtin_amdgcn_sched_group_barrier(0x002, 2, 0);
    }
    SCHED();
  }
  const __amdgpu_buffer_rsrc_t ro = __builtin_amdgcn_make_buffer_rsrc((void*)out, 0, M * N * 2, 0x00020000);
  const unsigned ooff = (unsigned)((m0 + 32 * w + 4 * q) * N + n0 + 4 * l15) * 2u;
#pragma unroll
  for (int mt = 0; mt < 2; ++mt)
#pragma unroll
    for (int reg = 0; reg < 4; ++reg) {
      v2u o;
#pragma unroll
      for (int cp = 0; cp < 2; ++cp) {
        const unsigned pk = cvt_pk_bf16((float)acc[mt][2 * cp][reg], (float)acc[mt][2 * cp + 1][reg]);
        const v2f a = {__uint_as_float(pk << 16), __uint_as_float(pk & 0xffff0000u)};
        const v2f s2 = {sv[2 * cp], sv[2 * cp + 1]}, o2 = {ov[2 * cp], ov[2 * cp + 1]};
        const v2f r = __builtin_elementwise_fma(a, s2, o2);
        o[cp] = cvt_pk_bf16(r[0], r[1]);
      }
      __builtin_amdgcn_raw_buffer_store_b64(o, ro, ooff, (mt * 16 + reg) * N * 2, AUX_OUT);
    }
}

extern "C" void kernel_launch(void* const* d_in, const int* in_sizes, int n_in, void* d_out, int out_size, void* d_ws,
                              size_t ws_size, hipStream_t stream) {
  (void)hipFuncSetAttribute((const void*)w8a8_kernel, hipFuncAttributeMaxDynamicSharedMemorySize, LDS_BYTES);
  unsigned char* ws = (unsigned char*)d_ws + ((ws_size - WS_BYTES) & ~(size_t)4095);
  w8a8_kernel<<<NWG, NTHR, LDS_BYTES, stream>>>((const int*)d_in[0], (const int*)d_in[1], ws, (unsigned short*)d_out,
                                                (const unsigned short*)d_in[2], (const int*)d_in[3],
                                                (const unsigned short*)d_in[4], (const unsigned short*)d_in[5]);
}
```

```cpp
#include <hip/hip_runtime.h>
#include <stdint.h>

typedef int v4i __attribute__((ext_vector_type(4)));
typedef unsigned v4u __attribute__((ext_vector_type(4)));
typedef unsigned v2u __attribute__((ext_vector_type(2)));
typedef float v2f __attribute__((ext_vector_type(2)));
typedef __bf16 v2b __attribute__((ext_vector_type(2)));
typedef unsigned long long u64;
#define GAS __attribute__((address_space(1)))
#define LAS __attribute__((address_space(3)))
#define RLX_AGENT __ATOMIC_RELAXED, __HIP_MEMORY_SCOPE_AGENT
#define SCHED() __builtin_amdgcn_sched_barrier(0)

constexpr int M = 512, K = 1024, N = 3584;
constexpr int NPACK = 32, NWG = 256, NTHR = 512;
constexpr int WS_X8 = 0;
constexpr int WS_FLAG = 512 * 1024;
constexpr int WS_EPOCH = WS_FLAG + 256 * 128;
constexpr int WS_BYTES = 576 * 1024;
constexpr int LDS_B = 0;
constexpr int LDS_CS = 65536;
constexpr int LDS_GO = LDS_CS + 256;
constexpr int LDS_BYTES = 84 * 1024;
constexpr int AUX_DEV = 16;
constexpr int AUX_OUT = 17;
constexpr int LAG = 3;
constexpr int WWIN = 12;
constexpr int POLL_GROUP = 10;
constexpr int XGO_GROUP = 12;
constexpr int XWIN = 3;
constexpr int BD = 2;

__device__ __forceinline__ unsigned pack4(unsigned a, unsigned b, unsigned c, unsigned d) {
  return __builtin_amdgcn_perm(b, a, 0x0c0c0400u) | __builtin_amdgcn_perm(d, c, 0x04000c0cu);
}
__device__ __forceinline__ unsigned cvt_pk_bf16(float a, float b) {
  v2f f = {a, b};
  return __builtin_bit_cast(unsigned, __builtin_convertvector(f, v2b));
}
__device__ __forceinline__ void bar() {
  __builtin_amdgcn_s_barrier();
  SCHED();
}
__device__ __forceinline__ void wait_flags(GAS u64* mine, bool sel, u64 tag) {
#pragma unroll 1
  for (unsigned spins = 0; spins < (1u << 18); ++spins) {
    if (sel) sel = __hip_atomic_load(mine, RLX_AGENT) != tag;
    if (!__any(sel)) break;
    __builtin_amdgcn_s_sleep(4);
  }
  __builtin_amdgcn_fence(__ATOMIC_ACQUIRE, "wavefront");
}

__global__ __launch_bounds__(NTHR, 2) void w8a8_kernel(const int* __restrict__ qx, const int* __restrict__ qw,
                                                       unsigned char* ws, unsigned short* __restrict__ out,
                                                       const unsigned short* __restrict__ scale_x,
                                                       const int* __restrict__ zero_x,
                                                       const unsigned short* __restrict__ scale_w,
                                                       const unsigned short* __restrict__ bias) {
  extern __shared__ __attribute__((aligned(16))) unsigned char smem[];
  const int tid = threadIdx.x;
  const int lane = tid & 63;
  const int w = __builtin_amdgcn_readfirstlane(tid >> 6) ^ 4;
  const int b = blockIdx.x;
  const int xcd = b & 7, idx = b >> 3;
  const int mq = idx & 3, grp = idx >> 2;
  const int m0 = mq * 128;
  const int l15 = lane & 15, q = lane >> 4;
  const bool cons = b >= NPACK;
  const int n0 = (xcd * 7 + grp - 1) * 64;
  const int j = grp * 8 + xcd;
  asm volatile("" ::"s"(qx), "s"(qw), "s"(ws), "s"(out));
  const __amdgpu_buffer_rsrc_t rs = __builtin_amdgcn_make_buffer_rsrc((void*)(ws + WS_X8), 0, M * K, 0x00020000);
  const __amdgpu_buffer_rsrc_t rw = __builtin_amdgcn_make_buffer_rsrc((void*)qw, 0, K * N * 4, 0x00020000);
  GAS u64* epoch = (GAS u64*)(ws + WS_EPOCH + mq * 256);
  GAS u64* flags = (GAS u64*)(ws + WS_FLAG + mq * 8192);
  volatile LAS unsigned* sync = (volatile LAS unsigned*)(smem + LDS_GO);
  const int w3 = w & 3;
  const bool loader = cons && w >= 4;
  const unsigned wl = (unsigned)((64 * w3 + 16 * q) * N + n0 + 4 * l15) * 4u;
  const int lcls = ((int)((uintptr_t)qw >> 7) + (n0 >> 5)) & 7;
  const int q0 = (lcls == 2 || lcls == 3) ? mq : 2 * (mq & 1);

  v4u raw[LAG + 1][4];
  if (loader) {
#pragma unroll
    for (int e = 0; e < WWIN; ++e)
      raw[e >> 2][e & 3] = __builtin_amdgcn_raw_buffer_load_b128(rw, wl + q0 * (256 * N * 4), e * N * 4, 0);
    asm volatile("" ::: "memory");
  }

  if (!cons) {
    const __amdgpu_buffer_rsrc_t rx = __builtin_amdgcn_make_buffer_rsrc((void*)qx, 0, M * K * 4, 0x00020000);
    const unsigned xoff = (unsigned)((m0 + 16 * xcd + (lane >> 5)) * K + 128 * w + 4 * (lane & 31)) * 4u;
    v4u xv[8];
#pragma unroll
    for (int i = 0; i < 8; ++i) xv[i] = __builtin_amdgcn_raw_buffer_load_b128(rx, xoff, i * 2 * K * 4, 0);
    SCHED();
    u64 tag = 0;
    if (w == 0) tag = __hip_atomic_load(epoch, RLX_AGENT) + 1ull;
    unsigned* xt = (unsigned*)(smem + w * 2304);
#pragma unroll
    for (int i = 0; i < 8; ++i)
      xt[(2 * i + (lane >> 5)) * 36 + (lane & 31)] = pack4(xv[i][0], xv[i][1], xv[i][2], xv[i][3]);
#pragma unroll
    for (int f = 0; f < 2; ++f) {
      const v4u p = *(const v4u*)(xt + l15 * 36 + 16 * f + 4 * q);
      __builtin_amdgcn_raw_buffer_store_b128(p, rs, (unsigned)(((mq * 8 + j) * 16 + 2 * w + f) * 1024 + lane * 16), 0,
                                             AUX_DEV);
    }
    asm volatile("s_waitcnt vmcnt(0)" ::: "memory");
    bar();
    if (w == 0) {
      if (lane == 0) __hip_atomic_store(flags + j * 16, tag, RLX_AGENT);
      if (j == 0) {
        __builtin_amdgcn_s_sleep(40);
        wait_flags(flags + lane * 16, true, tag);
        if (lane == 0) __hip_atomic_store(epoch, tag, RLX_AGENT);
      }
    }
    return;
  }

  if (w == 2) {
    if (lane < 8) sync[lane] = 0u;
    ((int*)(smem + LDS_CS))[lane] = 0;
    __builtin_amdgcn_s_waitcnt(0xC07F);
  }
  bar();

  if (loader) {
    v4u pk[4];
    int cs[4] = {0, 0, 0, 0};
    v4u* dst = (v4u*)(smem + LDS_B) + w3 * 256 + lane;
#pragma unroll
    for (int g = 0; g < 16 + LAG; ++g) {
      if (g < 16) {
        const unsigned off = wl + ((q0 + (g >> 2)) & 3) * (256 * N * 4);
#pragma unroll
        for (int e = 0; e < 4; ++e) {
          if (4 * g + e < WWIN) continue;
          asm volatile("s_waitcnt vmcnt(%0)" ::"n"(WWIN - 1) : "memory");
          raw[g % (LAG + 1)][e] = __builtin_amdgcn_raw_buffer_load_b128(rw, off, (4 * (g & 3) + e) * N * 4, 0);
          SCHED();
        }
      }
      if (g == POLL_GROUP) sync[4] = 1u;
      if (g == XGO_GROUP) sync[6] = 1u;
      if (g >= LAG) {
        const int p = g - LAG;
#pragma unroll
        for (int c = 0; c < 4; ++c) {
          pk[c][p & 3] = pack4(raw[p % (LAG + 1)][0][c], raw[p % (LAG + 1)][1][c], raw[p % (LAG + 1)][2][c],
                               raw[p % (LAG + 1)][3][c]);
          cs[c] = __builtin_amdgcn_sdot4((int)pk[c][p & 3], 0x01010101, cs[c], false);
        }
        if ((p & 3) == 3) {
          v4u* d2 = dst + ((q0 + (p >> 2)) & 3) * 1024;
#pragma unroll
          for (int c = 0; c < 4; ++c) d2[c * 64] = pk[c];
          if (p == 15) {
#pragma unroll
            for (int c = 0; c < 4; ++c) atomicAdd((int*)(smem + LDS_CS) + 4 * l15 + c, cs[c]);
          }
          asm volatile("s_waitcnt lgkmcnt(0)" ::: "memory");
          if (p == 15) {
            if (lane == 0) atomicAdd((unsigned*)(smem + LDS_GO) + 7, 1u);
          } else {
            sync[w3] = (unsigned)(p >> 2) + 1u;
          }
        }
        SCHED();
      }
    }
    return;
  }

  u64 tag = 0;
  if (w == 0) tag = __hip_atomic_load(epoch, RLX_AGENT) + 1ull;
  float sv[4], ov[4], zxf;
  {
    const v2u swv = *(const v2u*)&scale_w[n0 + 4 * l15];
    const v2u bv = *(const v2u*)&bias[n0 + 4 * l15];
    const float sx = __uint_as_float((unsigned)scale_x[0] << 16);
    const float z = (float)zero_x[0];
    zxf = __uint_as_float(cvt_pk_bf16(z, z) << 16);
#pragma unroll
    for (int c = 0; c < 4; ++c) {
      sv[c] = sx * __uint_as_float((c & 1) ? (swv[c >> 1] & 0xffff0000u) : (swv[c >> 1] << 16));
      ov[c] = __uint_as_float((c & 1) ? (bv[c >> 1] & 0xffff0000u) : (bv[c >> 1] << 16));
    }
  }
  unsigned xs[16], bs[16];
  {
    const int rot = (12 * (grp - 1)) / 7;
#pragma unroll
    for (int t = 0; t < 16; ++t) {
      const int ks = (4 * q0 + (t < 12 ? (t + rot >= 12 ? t + rot - 12 : t + rot) : 12 + ((t + grp) & 3))) & 15;
      xs[t] = (unsigned)(ks * 1024);
      bs[t] = (unsigned)(uintptr_t)((LAS unsigned char*)smem + LDS_B + ks * 4096 + lane * 16);
      asm volatile("" : "+s"(xs[t]), "+v"(bs[t]));
    }
  }
  SCHED();
  if (w == 0) {
    if (lane == 0) __hip_atomic_store(flags + j * 16, tag, RLX_AGENT);
    while (sync[4] != 1u) __builtin_amdgcn_s_sleep(2);
    wait_flags(flags + lane * 16, lane < 8, tag);
    sync[5] = 1u;
  }
  asm volatile("" : "+v"(sv[0]), "+v"(sv[1]), "+v"(sv[2]), "+v"(sv[3]), "+v"(ov[0]), "+v"(ov[1]), "+v"(ov[2]),
               "+v"(ov[3]), "+v"(zxf));
  while (sync[5] + sync[6] != 2u) __builtin_amdgcn_s_sleep(1);
  asm volatile("" ::: "memory");
  const unsigned x8off = (unsigned)((mq * 8 + 2 * w) * 16384 + lane * 16);
  v4u xa[16][2];
#pragma unroll
  for (int t = 0; t < XWIN; ++t)
#pragma unroll
    for (int mt = 0; mt < 2; ++mt)
      xa[t][mt] = __builtin_amdgcn_raw_buffer_load_b128(rs, x8off + mt * 16384, xs[t], AUX_DEV);
  SCHED();
  v4i acc[2][4];
#pragma unroll
  for (int mt = 0; mt < 2; ++mt)
#pragma unroll
    for (int c = 0; c < 4; ++c) acc[mt][c] = (v4i){0, 0, 0, 0};
  asm volatile("" : "+v"(acc[0][0]), "+v"(acc[0][1]), "+v"(acc[0][2]), "+v"(acc[0][3]), "+v"(acc[1][0]), "+v"(acc[1][1]),
               "+v"(acc[1][2]), "+v"(acc[1][3]));
  for (;;) {
    const v4u pr = *(volatile LAS v4u*)sync;
    if (__builtin_amdgcn_readfirstlane(min(min(pr[0], pr[1]), min(pr[2], pr[3]))) >= 3u) break;
    __builtin_amdgcn_s_sleep(1);
  }
  asm volatile("" ::: "memory");
  v4u bb[BD + 1][4];
#pragma unroll
  for (int t = 0; t < BD; ++t)
#pragma unroll
    for (int c = 0; c < 4; ++c) bb[t][c] = *(const LAS v4u*)(uintptr_t)(bs[t] + c * 1024);
  SCHED();
  v4i csv;
  unsigned done;
  float tz[4] = {0.f, 0.f, 0.f, 0.f};
#pragma unroll
  for (int t = 0; t < 16; ++t) {
    if (t == 12 - BD) {
      if (__builtin_amdgcn_readfirstlane(done) < 4u) {
        while (sync[7] < 4u) __builtin_amdgcn_s_sleep(1);
      }
      asm volatile("" ::: "memory");
      csv = *(const v4i*)(smem + LDS_CS + 16 * l15);
      SCHED();
    }
    if (t == 11 - BD) {
      done = sync[7];
      SCHED();
    }
#pragma unroll
    for (int i = 0; i < 8; ++i) {
      asm volatile("v_mfma_i32_16x16x64_i8 %0, %1, %2, %0"
                   : "+v"(acc[i >> 2][i & 3])
                   : "v"(xa[t][i >> 2]), "v"(bb[t % (BD + 1)][i & 3]));
      SCHED();
      if (i < 4) {
        if (t + BD < 16) bb[(t + BD) % (BD + 1)][i] = *(const LAS v4u*)(uintptr_t)(bs[t + BD] + i * 1024);
      } else if (i == 4 || i == 6) {
        if (t + XWIN < 16)
          xa[t + XWIN][(i - 4) >> 1] =
              __builtin_amdgcn_raw_buffer_load_b128(rs, x8off + ((i - 4) >> 1) * 16384, xs[t + XWIN], AUX_DEV);
      }
      if (t >= 14) {
        const int sidx = (t - 14) * 8 + i, st = sidx >> 1, c0 = (sidx & 1) * 2;
#pragma unroll
        for (int c = c0; c < c0 + 2; ++c) {
          if (st == 0) tz[c] = (float)csv[c];
          if (st == 1 || st == 4) tz[c] = __uint_as_float(cvt_pk_bf16(tz[c], tz[c]));
          if (st == 2 || st == 5) tz[c] = __uint_as_float(__float_as_uint(tz[c]) << 16);
          if (st == 3) tz[c] = zxf * tz[c];
          if (st == 6) ov[c] = __builtin_fmaf(-tz[c], sv[c], ov[c]);
          asm volatile("" : "+v"(tz[c]), "+v"(ov[c]));
        }
      }
      SCHED();
    }
  }
  asm volatile("s_nop 7\n\ts_nop 1"
               : "+v"(acc[0][0]), "+v"(acc[0][1]), "+v"(acc[0][2]), "+v"(acc[0][3]), "+v"(acc[1][0]), "+v"(acc[1][1]),
                 "+v"(acc[1][2]), "+v"(acc[1][3]));
  SCHED();
  const __amdgpu_buffer_rsrc_t ro = __builtin_amdgcn_make_buffer_rsrc((void*)out, 0, M * N * 2, 0x00020000);
  const unsigned ooff = (unsigned)((m0 + 32 * w + 4 * q) * N + n0 + 4 * l15) * 2u;
#pragma unroll
  for (int mt = 0; mt < 2; ++mt)
#pragma unroll
    for (int reg = 0; reg < 4; ++reg) {
      v2u o;
#pragma unroll
      for (int cp = 0; cp < 2; ++cp) {
        const unsigned pk = cvt_pk_bf16((float)acc[mt][2 * cp][reg], (float)acc[mt][2 * cp + 1][reg]);
        const v2f a = {__uint_as_float(pk << 16), __uint_as_float(pk & 0xffff0000u)};
        const v2f s2 = {sv[2 * cp], sv[2 * cp + 1]}, o2 = {ov[2 * cp], ov[2 * cp + 1]};
        const v2f r = __builtin_elementwise_fma(a, s2, o2);
        o[cp] = cvt_pk_bf16(r[0], r[1]);
      }
      __builtin_amdgcn_raw_buffer_store_b64(o, ro, ooff, (mt * 16 + reg) * N * 2, AUX_OUT);
    }
}

extern "C" void kernel_launch(void* const* d_in, const int* in_sizes, int n_in, void* d_out, int out_size, void* d_ws,
                              size_t ws_size, hipStream_t stream) {
  (void)hipFuncSetAttribute((const void*)w8a8_kernel, hipFuncAttributeMaxDynamicSharedMemorySize, LDS_BYTES);
  unsigned char* ws = (unsigned char*)d_ws + ((ws_size - WS_BYTES) & ~(size_t)4095);
  w8a8_kernel<<<NWG, NTHR, LDS_BYTES, stream>>>((const int*)d_in[0], (const int*)d_in[1], ws, (unsigned short*)d_out,
                                                (const unsigned short*)d_in[2], (const int*)d_in[3],
                                                (const unsigned short*)d_in[4], (const unsigned short*)d_in[5]);
}
```
